```python
import torch

def _softmax(x, axis=-1):
    return torch.softmax(x, dim=axis)
def _gelu(x):
    return torch.nn.functional.gelu(x, approximate="tanh")
def _silu(x):
    return torch.nn.functional.silu(x)
def _sigmoid(x):
    return torch.sigmoid(x)
def _layernorm(x, axis=-1, eps=1e-5):
    m = x.mean(dim=axis, keepdim=True)
    v = x.var(dim=axis, keepdim=True, unbiased=False)
    return (x - m) / torch.sqrt(v + eps)
def _scatter_add(tgt, idx, src, axis=0):
    return tgt.clone().index_add_(axis, idx, src)
def _segment_sum(src, seg, n):
    out = torch.zeros((n,) + src.shape[1:], dtype=src.dtype)
    return out.index_add_(0, seg, src)
def _pad(x, pw):
    flat = tuple(v for pair in reversed(pw) for v in pair)
    return torch.nn.functional.pad(x, flat)

kernel_name = 'standalone__grouped__vision'

def setup_inputs():

    _w = (lambda a: (lambda t: t.to(torch.bfloat16) if t.is_floating_point() else t)(torch.as_tensor(a)))
    H = W = 384
    G = 4
    Cg = max(96 // G, 2)
    C = G * Cg
    k = 3
    scale = 1.0 / (k * Cg ** 0.5)
    return {
        "x": _w((lambda *s: torch.randn(*s, dtype=torch.bfloat16) * 0.05)(1, H, W, C)),
        "w": _w((lambda *s: torch.randn(*s, dtype=torch.bfloat16) * 0.05)(k, k, G, Cg, Cg) * scale),
    }

def reference(x, w):
    B, H, W, C = x.shape
    k = w.shape[0]
    G = w.shape[2]
    Cg = C // G
    pad = k // 2
    xp = _pad(x, ((0,0),(pad,pad),(pad,pad),(0,0)))
    xg = xp.reshape(B, H + 2 * pad, W + 2 * pad, G, Cg)
    out = torch.zeros_like(x).reshape(B, H, W, G, Cg)
    for i in range(k):
        for j in range(k):
            out = out + torch.einsum('bhwgc,gcd->bhwgd', xg[:, i:i+H, j:j+W, :, :], w[i, j])
    return out.reshape(B, H, W, C)
```

```cpp
#include <hip/hip_runtime.h>
#include <hip/hip_bf16.h>

typedef __attribute__((ext_vector_type(8))) __bf16 bf16x8;
typedef __attribute__((ext_vector_type(4))) __bf16 bf16x4;
typedef __attribute__((ext_vector_type(2))) __bf16 bf16x2;
typedef __attribute__((ext_vector_type(4))) short s16x4;
typedef __attribute__((ext_vector_type(4))) float f32x4;
typedef __attribute__((ext_vector_type(2))) float f32x2;
typedef __attribute__((ext_vector_type(4))) unsigned u32x4;
typedef __attribute__((ext_vector_type(2))) unsigned u32x2;
typedef __attribute__((address_space(3))) char lds_char;

constexpr int IMG = 384;
constexpr int ROWB = IMG * 192;
constexpr int TW = 48, TH = 12;
constexpr int NROW = TH + 2;
constexpr int NP = TH / 2;
constexpr int LROW = 650 * 16;
constexpr int LDS_BYTES = NROW * LROW;
constexpr int WBYTES = 9 * 4 * 24 * 24 * 2;
constexpr int WOFF = 10 * LROW;
constexpr int ZOFF = WOFF + WBYTES;
constexpr unsigned OOB = 0x80000000u;
constexpr int ST_NT = 2;

constexpr int ST[NP] = {0, 0, 0, 4, 2, 2};
constexpr int st_done(int p) { int d = 0; for (int s = 0; s <= p; ++s) d += ST[s]; return d; }
constexpr int st_first(int p) { return p == 0 ? 0 : st_done(p - 1); }
constexpr int mid_row(int p) { return p < 3 ? 8 + 2 * p : -1; }
constexpr int end_row(int p) { return p < 3 ? 9 + 2 * p : -1; }
constexpr int younger(int p) {
    const int need = 2 * p + 5;
    int cnt = 0;
    for (int r = need + 1; r < 8; ++r) ++cnt;
    bool seen = need < 8;
    for (int s = 0; s <= p; ++s) {
        if (mid_row(s) >= 0) { if (seen) ++cnt; if (mid_row(s) == need) seen = true; }
        if (seen) cnt += ST[s];
        if (end_row(s) >= 0) { if (seen) ++cnt; if (end_row(s) == need) seen = true; }
    }
    return cnt;
}

__device__ __forceinline__ void dma16(unsigned m0v, unsigned voff, __amdgpu_buffer_rsrc_t rs, unsigned long long mask) {
    asm volatile("s_mov_b32 m0, %0\n\ts_mov_b64 exec, %3\n\tbuffer_load_dwordx4 %1, %2, 0 offen lds\n\ts_mov_b64 exec, -1"
                 :: "s"(m0v), "v"(voff), "s"(rs), "s"(mask) : "memory");
}
__device__ __forceinline__ bf16x8 lds_frag(const lds_char* p) {
    return *(const __attribute__((address_space(3))) bf16x8*)p;
}
__device__ __forceinline__ u32x4 lds_u32x4(const lds_char* p) {
    return *(const __attribute__((address_space(3))) u32x4*)p;
}
__device__ __forceinline__ bf16x4 trread(const lds_char* p) {
    return __builtin_bit_cast(bf16x4, __builtin_amdgcn_ds_read_tr16_b64_v4i16((__attribute__((address_space(3))) s16x4*)p));
}
__device__ __forceinline__ bf16x8 trfrag(const lds_char* lo, const lds_char* hi) {
    return __builtin_shufflevector(trread(lo), trread(hi), 0, 1, 2, 3, 4, 5, 6, 7);
}
__device__ __forceinline__ f32x4 mma(bf16x8 a, bf16x8 b, f32x4 c) {
    return __builtin_amdgcn_mfma_f32_16x16x32_bf16(a, b, c, 0, 0, 0);
}
__device__ __forceinline__ unsigned pk(float a, float b) {
    f32x2 v = {a, b};
    return __builtin_bit_cast(unsigned, __builtin_convertvector(v, bf16x2));
}
__device__ __forceinline__ void stage(lds_char* p, f32x4 a) {
    *(__attribute__((address_space(3))) u32x2*)p = u32x2{pk(a[0], a[1]), pk(a[2], a[3])};
}

struct Ctx {
    bf16x8 AL[3][2], AL2[2];
    bf16x8 AH[4][2], AH2;
    bf16x8 Ra[4], Rb[4];
    f32x4 pL0, pL1, pH;
    const lds_char *pF0, *pF1, *pF2, *srd;
    lds_char *swL, *swH;
    unsigned voff, ovoff, m0x;
    int osoff, ystep;
    unsigned long long xmask;
    __amdgpu_buffer_rsrc_t rx, ro;
};

__device__ __forceinline__ void load_row(Ctx& c, int r) { dma16(c.m0x + r * LROW, c.voff, c.rx, c.xmask); c.voff += c.ystep; }
__device__ __forceinline__ void store_row(Ctx& c, u32x4 v) {
    __builtin_amdgcn_raw_buffer_store_b128(v, c.ro, c.ovoff, c.osoff, ST_NT);
    c.osoff += c.ystep;
}

template <int P> __device__ __forceinline__ void pair_step(Ctx& c) {
    constexpr int O = 2 * P;
    constexpr int ra = O % 4, rb = (O + 1) % 4, rc = (O + 2) % 4, rd = (O + 3) % 4;
    c.Ra[rc] = lds_frag(c.pF0 + (O + 2) * LROW);
    c.Rb[rc] = lds_frag(c.pF1 + (O + 2) * LROW);
    c.Ra[rd] = lds_frag(c.pF0 + (O + 3) * LROW);
    c.Rb[rd] = lds_frag(c.pF1 + (O + 3) * LROW);
    const bf16x8 F2 = lds_frag(c.pF2 + O * LROW);
    if constexpr (P >= 1) {
        stage(c.swL + (O - 2) * LROW, c.pL0);
        stage(c.swL + (O - 1) * LROW, c.pL1);
        stage(c.swH + (O - 2) * LROW, c.pH);
    }
    constexpr int NS = ST[P], S0 = st_first(P);
    static_assert(S0 + NS <= (P >= 1 ? 2 * P - 2 : 0), "stored rows must have been staged before the last barrier");
    u32x4 ov[NS > 0 ? NS : 1];
#pragma unroll
    for (int k = 0; k < NS; ++k) ov[k] = lds_u32x4(c.srd + (S0 + k) * LROW);

    f32x4 l0 = {0, 0, 0, 0}, l1 = {0, 0, 0, 0}, h = {0, 0, 0, 0};
    l0 = mma(c.AL[0][0], c.Ra[ra], l0); h = mma(c.AH[0][0], c.Ra[ra], h);
    l1 = mma(c.AL[0][0], c.Ra[rb], l1);
    l0 = mma(c.AL[0][1], c.Rb[ra], l0); h = mma(c.AH[0][1], c.Rb[ra], h);
    l1 = mma(c.AL[0][1], c.Rb[rb], l1);
    l0 = mma(c.AL[1][0], c.Ra[rb], l0); h = mma(c.AH[1][0], c.Ra[rb], h);
    l0 = mma(c.AL[1][1], c.Rb[rb], l0); h = mma(c.AH[1][1], c.Rb[rb], h);
    if constexpr (mid_row(P) >= 0 || NS > 0) {
        asm volatile("" : "+v"(l0), "+v"(l1), "+v"(h));
        if constexpr (mid_row(P) >= 0) load_row(c, mid_row(P));
        if constexpr (NS > 0) {
#pragma unroll
            for (int k = 0; k < NS; ++k) store_row(c, ov[k]);
            asm volatile("" ::: "memory");
        }
    }
    l1 = mma(c.AL[1][0], c.Ra[rc], l1); l0 = mma(c.AL[2][0], c.Ra[rc], l0); h = mma(c.AH[2][0], c.Ra[rc], h);
    l1 = mma(c.AL[1][1], c.Rb[rc], l1); l0 = mma(c.AL[2][1], c.Rb[rc], l0); h = mma(c.AH[2][1], c.Rb[rc], h);
    l1 = mma(c.AL[2][0], c.Ra[rd], l1); h = mma(c.AH[3][0], c.Ra[rd], h);
    l1 = mma(c.AL[2][1], c.Rb[rd], l1); h = mma(c.AH[3][1], c.Rb[rd], h);
    l0 = mma(c.AL2[0], F2, l0); l1 = mma(c.AL2[1], F2, l1); h = mma(c.AH2, F2, h);
    asm volatile("" : "+v"(l0), "+v"(l1), "+v"(h));
    c.pL0 = l0; c.pL1 = l1; c.pH = h;
    if constexpr (end_row(P) >= 0) load_row(c, end_row(P));
    if constexpr (P + 1 < NP) {
        static_assert(younger(P) < 64, "vmcnt range");
        asm volatile("s_waitcnt vmcnt(%0) lgkmcnt(0)\n\ts_barrier" :: "i"(younger(P)) : "memory");
        __builtin_amdgcn_sched_barrier(0);
    }
}

__global__ __launch_bounds__(768) void gconv_kernel(const void* __restrict__ x, const void* __restrict__ w,
                                                    void* __restrict__ out) {
    __shared__ __attribute__((aligned(16))) char smem_[LDS_BYTES];
    lds_char* smem = (lds_char*)smem_;
    const unsigned ldsb = (unsigned)(size_t)smem;
    Ctx c;

    const int tid = threadIdx.x;
    const int lane = tid & 63;
    const int wv = __builtin_amdgcn_readfirstlane(tid >> 6);
    const int bid = blockIdx.x;
    const int trow = (bid & 7) * 4 + (bid >> 6);
    const int tcol = (bid >> 3) & 7;
    const int x0 = tcol * TW, y0 = trow * TH;
    const bool flip = !(trow & 1);
    c.ystep = flip ? -ROWB : ROWB;

    c.rx = __builtin_amdgcn_make_buffer_rsrc((void*)x, 0, IMG * ROWB, 0x00020000);
    const __amdgpu_buffer_rsrc_t rw = __builtin_amdgcn_make_buffer_rsrc((void*)w, 0, WBYTES, 0x00020000);
    c.ro = __builtin_amdgcn_make_buffer_rsrc(out, 0, IMG * ROWB, 0x00020000);

    const int slot = wv * 55 + lane;
    const int spx = (slot * 5042) >> 16;
    const int sc = slot - spx * 13;
    const int gx = x0 - 1 + spx;
    c.voff = (sc < 12 && gx >= 0 && gx < IMG) ? (unsigned)((flip ? y0 + TH : y0 - 1) * ROWB + gx * 192 + sc * 16) : OOB;
    c.xmask = wv < 11 ? (1ull << 55) - 1 : (1ull << 45) - 1;
    c.m0x = ldsb + wv * 880;
#pragma unroll
    for (int i = 0; i < 4; ++i)
        dma16(ldsb + WOFF + (wv * 216 + i * 54) * 16, (unsigned)((wv * 216 + i * 54 + lane) * 16), rw,
              (i == 3 && wv == 11) ? (1ull << 55) - 1 : (1ull << 54) - 1);
    asm volatile("s_barrier" ::: "memory");
#pragma unroll
    for (int r = 0; r < 4; ++r) load_row(c, r);

    const int g = wv & 3, ct = wv >> 2;
    const int n = lane & 15, q = lane >> 4;
    const int px = 2 * (n & 3) + 8 * (n >> 3) + (((n >> 2) ^ (n >> 3)) & 1);
    const int pbase = ((ct * 16 + px) * 13 + 3 * g) * 16;
    const int c0 = q == 0 ? 0 : q == 1 ? 2 : q == 2 ? 14 : 26;
    const int c1 = q == 0 ? 1 : q == 1 ? 13 : q == 2 ? 15 : 27;
    c.pF0 = smem + pbase + c0 * 16;
    c.pF1 = smem + pbase + c1 * 16;
    c.pF2 = smem + pbase + 28 * 16 + q * LROW;
    c.swL = smem + pbase + 8 * q;
    c.swH = smem + pbase + 32 + 8 * (q & 1) + (q >> 1) * LROW;
    const int rj = lane < 48 ? lane : 47;
    const int rpx = (rj * 43) >> 9;
    c.srd = smem + ((wv * 4 + rpx) * 13 + (rj - rpx * 12)) * 16;
    c.ovoff = lane < 48 ? (unsigned)((x0 + wv * 4) * 192 + lane * 16) : OOB;
    c.osoff = (flip ? y0 + TH - 1 : y0) * ROWB;
    const int qq = (lane >> 2) & 3, pp = lane & 3;
    const int dx0 = c0 / 13, sb0 = c0 - dx0 * 13, dx1 = c1 / 13, sb1 = c1 - dx1 * 13;
    const int wdy = flip ? -13824 : 13824;
    const int wl = WOFF + g * 1152 + qq * 48 + (flip ? 2 * 13824 : 0);
    const int t0 = dx0 * 4608 + sb0 * 384, t1 = dx1 * 4608 + sb1 * 384, t2 = 2 * 4608 + 2 * 384;
    const bool up = pp >= 2;
    const int hoff = 32 + (pp & 1) * 8 - (pp >> 1) * wdy;
    int aL[3][2], aH[2][2], zA[7][2];
#pragma unroll
    for (int dy = 0; dy < 3; ++dy) { aL[dy][0] = wl + t0 + pp * 8 + dy * wdy; aL[dy][1] = wl + t1 + pp * 8 + dy * wdy; }
#pragma unroll
    for (int i = 0; i < 2; ++i) { aH[i][0] = wl + t0 + hoff + (i + 1) * wdy; aH[i][1] = wl + t1 + hoff + (i + 1) * wdy; }
    {
        const int b[7] = {wl + t2 + pp * 8 + q * wdy, wl + t2 + pp * 8 + (q - 1) * wdy,
                          wl + t0 + hoff, wl + t1 + hoff, wl + t0 + hoff + 3 * wdy, wl + t1 + hoff + 3 * wdy,
                          wl + t2 + hoff + q * wdy};
        const bool ok[7] = {q < 3, q > 0, !up, !up, up, up, up ? q > 0 : q < 3};
#pragma unroll
        for (int i = 0; i < 7; ++i) { zA[i][0] = ok[i] ? b[i] : ZOFF; zA[i][1] = ok[i] ? b[i] + 192 : ZOFF; }
    }
#pragma unroll
    for (int i = 0; i < 7; ++i) asm volatile("" : "+v"(zA[i][0]), "+v"(zA[i][1]));
#pragma unroll
    for (int i = 0; i < 3; ++i) asm volatile("" : "+v"(aL[i][0]), "+v"(aL[i][1]));
#pragma unroll
    for (int i = 0; i < 2; ++i) asm volatile("" : "+v"(aH[i][0]), "+v"(aH[i][1]));

    asm volatile("s_waitcnt vmcnt(4)\n\ts_barrier" ::: "memory");
#pragma unroll
    for (int f = 0; f < 2; ++f) {
        c.AL[0][f] = trfrag(smem + aL[0][f], smem + aL[0][f] + 192);
        c.AH[0][f] = trfrag(smem + zA[2 + f][0], smem + zA[2 + f][1]);
        c.AL[1][f] = trfrag(smem + aL[1][f], smem + aL[1][f] + 192);
        c.AH[1][f] = trfrag(smem + aH[0][f], smem + aH[0][f] + 192);
    }
#pragma unroll
    for (int f = 0; f < 2; ++f) {
        c.AL[2][f] = trfrag(smem + aL[2][f], smem + aL[2][f] + 192);
        c.AH[2][f] = trfrag(smem + aH[1][f], smem + aH[1][f] + 192);
        c.AH[3][f] = trfrag(smem + zA[4 + f][0], smem + zA[4 + f][1]);
    }
    c.AL2[0] = trfrag(smem + zA[0][0], smem + zA[0][1]);
    c.AL2[1] = trfrag(smem + zA[1][0], smem + zA[1][1]);
    c.AH2 = trfrag(smem + zA[6][0], smem + zA[6][1]);
    asm volatile("" ::: "memory");
    load_row(c, 4);
    load_row(c, 5);

    asm volatile("s_waitcnt vmcnt(2)\n\ts_barrier" ::: "memory");
    load_row(c, 6);
    c.Ra[0] = lds_frag(c.pF0); c.Rb[0] = lds_frag(c.pF1);
    c.Ra[1] = lds_frag(c.pF0 + LROW); c.Rb[1] = lds_frag(c.pF1 + LROW);
    load_row(c, 7);

    pair_step<0>(c); pair_step<1>(c); pair_step<2>(c); pair_step<3>(c); pair_step<4>(c); pair_step<5>(c);

    static_assert(st_done(NP - 2) >= 2, "slots 0 and 1 must be drained");
    stage(c.swL, c.pL0);
    stage(c.swL + LROW, c.pL1);
    stage(c.swH, c.pH);
    asm volatile("s_waitcnt lgkmcnt(0)\n\ts_barrier" ::: "memory");
    constexpr int S0 = st_done(NP - 1), NS = TH - S0;
    u32x4 v[NS];
#pragma unroll
    for (int k = 0; k < NS; ++k) v[k] = lds_u32x4(c.srd + (S0 + k >= TH - 2 ? S0 + k - (TH - 2) : S0 + k) * LROW);
#pragma unroll
    for (int k = 0; k < NS; ++k) store_row(c, v[k]);
}

extern "C" void kernel_launch(void* const* d_in, const int* in_sizes, int n_in,
                              void* d_out, int out_size, void* d_ws, size_t ws_size,
                              hipStream_t stream) {
    gconv_kernel<<<256, 768, 0, stream>>>(d_in[0], d_in[1], d_out);
}
```

```cpp
#include <hip/hip_runtime.h>
#include <hip/hip_bf16.h>

typedef __attribute__((ext_vector_type(8))) __bf16 bf16x8;
typedef __attribute__((ext_vector_type(4))) __bf16 bf16x4;
typedef __attribute__((ext_vector_type(2))) __bf16 bf16x2;
typedef __attribute__((ext_vector_type(4))) short s16x4;
typedef __attribute__((ext_vector_type(4))) float f32x4;
typedef __attribute__((ext_vector_type(2))) float f32x2;
typedef __attribute__((ext_vector_type(4))) unsigned u32x4;
typedef __attribute__((ext_vector_type(2))) unsigned u32x2;
typedef __attribute__((address_space(3))) char lds_char;

constexpr int IMG = 384;
constexpr int ROWB = IMG * 192;
constexpr int TW = 48, TH = 12;
constexpr int NROW = TH + 2;
constexpr int NP = TH / 2;
constexpr int LROW = 650 * 16;
constexpr int WBYTES = 9 * 4 * 24 * 24 * 2;
constexpr int WOFF = 10 * LROW;
constexpr int ZOFF = WOFF + WBYTES;
constexpr int LDS_BYTES = WOFF + 48 * 1024;
constexpr unsigned OOB = 0x80000000u;
constexpr int ST_NT = 2;

constexpr int ST[NP] = {0, 0, 0, 4, 2, 2};
constexpr int st_done(int p) { int d = 0; for (int s = 0; s <= p; ++s) d += ST[s]; return d; }
constexpr int st_first(int p) { return p == 0 ? 0 : st_done(p - 1); }
constexpr int mid_row(int p) { return p < 3 ? 8 + 2 * p : -1; }
constexpr int end_row(int p) { return p < 3 ? 9 + 2 * p : -1; }
constexpr int younger(int p) {
    const int need = 2 * p + 5;
    int cnt = 0;
    for (int r = need + 1; r < 8; ++r) ++cnt;
    bool seen = need < 8;
    for (int s = 0; s <= p; ++s) {
        if (mid_row(s) >= 0) { if (seen) ++cnt; if (mid_row(s) == need) seen = true; }
        if (seen) cnt += ST[s];
        if (end_row(s) >= 0) { if (seen) ++cnt; if (end_row(s) == need) seen = true; }
    }
    return cnt;
}

__device__ __forceinline__ void dma16(unsigned m0v, unsigned voff, __amdgpu_buffer_rsrc_t rs, unsigned long long mask) {
    asm volatile("s_mov_b32 m0, %0\n\ts_mov_b64 exec, %3\n\tbuffer_load_dwordx4 %1, %2, 0 offen lds\n\ts_mov_b64 exec, -1"
                 :: "s"(m0v), "v"(voff), "s"(rs), "s"(mask) : "memory");
}
__device__ __forceinline__ bf16x8 lds_frag(const lds_char* p) {
    return *(const __attribute__((address_space(3))) bf16x8*)p;
}
__device__ __forceinline__ u32x4 lds_u32x4(const lds_char* p) {
    return *(const __attribute__((address_space(3))) u32x4*)p;
}
__device__ __forceinline__ bf16x4 trread(const lds_char* p) {
    return __builtin_bit_cast(bf16x4, __builtin_amdgcn_ds_read_tr16_b64_v4i16((__attribute__((address_space(3))) s16x4*)p));
}
__device__ __forceinline__ bf16x8 trfrag(const lds_char* p) {
    return __builtin_shufflevector(trread(p), trread(p + 192), 0, 1, 2, 3, 4, 5, 6, 7);
}
__device__ __forceinline__ f32x4 mma(bf16x8 a, bf16x8 b, f32x4 c) {
    return __builtin_amdgcn_mfma_f32_16x16x32_bf16(a, b, c, 0, 0, 0);
}
__device__ __forceinline__ unsigned pk(float a, float b) {
    f32x2 v = {a, b};
    return __builtin_bit_cast(unsigned, __builtin_convertvector(v, bf16x2));
}
__device__ __forceinline__ void stage(lds_char* p, f32x4 a) {
    *(__attribute__((address_space(3))) u32x2*)p = u32x2{pk(a[0], a[1]), pk(a[2], a[3])};
}

struct Ctx {
    bf16x8 AL[3][2], AL2[2];
    bf16x8 AH[4][2], AH2;
    bf16x8 Ra[4], Rb[4];
    f32x4 pL0, pL1, pH;
    const lds_char *pF0, *pF1, *pF2, *srd;
    lds_char *swL, *swH;
    unsigned voff, ovoff, m0x;
    int osoff, ystep;
    unsigned long long xmask;
    __amdgpu_buffer_rsrc_t rx, ro;
};

__device__ __forceinline__ void load_row(Ctx& c, int r) { dma16(c.m0x + r * LROW, c.voff, c.rx, c.xmask); c.voff += c.ystep; }
__device__ __forceinline__ void store_row(Ctx& c, u32x4 v) {
    __builtin_amdgcn_raw_buffer_store_b128(v, c.ro, c.ovoff, c.osoff, ST_NT);
    c.osoff += c.ystep;
}

template <int P> __device__ __forceinline__ void pair_step(Ctx& c) {
    constexpr int O = 2 * P;
    constexpr int ra = O % 4, rb = (O + 1) % 4, rc = (O + 2) % 4, rd = (O + 3) % 4;
    c.Ra[rc] = lds_frag(c.pF0 + (O + 2) * LROW);
    c.Rb[rc] = lds_frag(c.pF1 + (O + 2) * LROW);
    c.Ra[rd] = lds_frag(c.pF0 + (O + 3) * LROW);
    c.Rb[rd] = lds_frag(c.pF1 + (O + 3) * LROW);
    const bf16x8 F2 = lds_frag(c.pF2 + O * LROW);
    if constexpr (P >= 1) {
        stage(c.swL + (O - 2) * LROW, c.pL0);
        stage(c.swL + (O - 1) * LROW, c.pL1);
        stage(c.swH + (O - 2) * LROW, c.pH);
    }
    constexpr int NS = ST[P], S0 = st_first(P);
    static_assert(S0 + NS <= (P >= 1 ? 2 * P - 2 : 0), "stored rows must have been staged before the last barrier");
    u32x4 ov[NS > 0 ? NS : 1];
#pragma unroll
    for (int k = 0; k < NS; ++k) ov[k] = lds_u32x4(c.srd + (S0 + k) * LROW);

    f32x4 l0 = {0, 0, 0, 0}, l1 = {0, 0, 0, 0}, h = {0, 0, 0, 0};
    l0 = mma(c.AL[0][0], c.Ra[ra], l0); h = mma(c.AH[0][0], c.Ra[ra], h);
    l1 = mma(c.AL[0][0], c.Ra[rb], l1);
    l0 = mma(c.AL[0][1], c.Rb[ra], l0); h = mma(c.AH[0][1], c.Rb[ra], h);
    l1 = mma(c.AL[0][1], c.Rb[rb], l1);
    l0 = mma(c.AL[1][0], c.Ra[rb], l0); h = mma(c.AH[1][0], c.Ra[rb], h);
    l0 = mma(c.AL[1][1], c.Rb[rb], l0); h = mma(c.AH[1][1], c.Rb[rb], h);
    if constexpr (mid_row(P) >= 0 || NS > 0) {
        asm volatile("" : "+v"(l0), "+v"(l1), "+v"(h));
        if constexpr (mid_row(P) >= 0) load_row(c, mid_row(P));
        if constexpr (NS > 0) {
#pragma unroll
            for (int k = 0; k < NS; ++k) store_row(c, ov[k]);
            asm volatile("" ::: "memory");
        }
    }
    l1 = mma(c.AL[1][0], c.Ra[rc], l1); l0 = mma(c.AL[2][0], c.Ra[rc], l0); h = mma(c.AH[2][0], c.Ra[rc], h);
    l1 = mma(c.AL[1][1], c.Rb[rc], l1); l0 = mma(c.AL[2][1], c.Rb[rc], l0); h = mma(c.AH[2][1], c.Rb[rc], h);
    l1 = mma(c.AL[2][0], c.Ra[rd], l1); h = mma(c.AH[3][0], c.Ra[rd], h);
    l1 = mma(c.AL[2][1], c.Rb[rd], l1); h = mma(c.AH[3][1], c.Rb[rd], h);
    l0 = mma(c.AL2[0], F2, l0); l1 = mma(c.AL2[1], F2, l1); h = mma(c.AH2, F2, h);
    asm volatile("" : "+v"(l0), "+v"(l1), "+v"(h));
    c.pL0 = l0; c.pL1 = l1; c.pH = h;
    if constexpr (end_row(P) >= 0) load_row(c, end_row(P));
    if constexpr (P + 1 < NP) {
        static_assert(younger(P) < 64, "vmcnt range");
        asm volatile("s_waitcnt vmcnt(%0) lgkmcnt(0)\n\ts_barrier" :: "i"(younger(P)) : "memory");
        __builtin_amdgcn_sched_barrier(0);
    }
}

__global__ __launch_bounds__(768) void gconv_kernel(const void* __restrict__ x, const void* __restrict__ w,
                                                    void* __restrict__ out) {
    __shared__ __attribute__((aligned(16))) char smem_[LDS_BYTES];
    lds_char* smem = (lds_char*)smem_;
    const unsigned ldsb = (unsigned)(size_t)smem;
    Ctx c;

    const int tid = threadIdx.x;
    int lane = tid & 63;
    const int wv = __builtin_amdgcn_readfirstlane(tid >> 6);

    {
        unsigned wm0 = ldsb + WOFF + wv * 4096;
        unsigned wvo = (unsigned)(wv * 4096 + lane * 16);
        asm volatile("" : "+s"(wm0), "+v"(wvo));
        const __amdgpu_buffer_rsrc_t rw = __builtin_amdgcn_make_buffer_rsrc((void*)w, 0, WBYTES, 0x00020000);
        asm volatile("s_mov_b32 m0, %0\n\ts_nop 0\n\t"
                     "buffer_load_dwordx4 %1, %2, 0 offen lds\n\t"
                     "buffer_load_dwordx4 %1, %2, 0 offen offset:1024 lds\n\t"
                     "buffer_load_dwordx4 %1, %2, 0 offen offset:2048 lds\n\t"
                     "buffer_load_dwordx4 %1, %2, 0 offen offset:3072 lds"
                     :: "s"(wm0), "v"(wvo), "s"(rw) : "memory");
    }
    int bid = blockIdx.x;
    asm volatile("" : "+s"(bid));

    const int trow = (bid & 7) * 4 + (bid >> 6);
    const int x0 = ((bid >> 3) & 7) * TW, y0 = trow * TH;
    const bool flip = !(trow & 1);
    c.ystep = flip ? -ROWB : ROWB;
    c.rx = __builtin_amdgcn_make_buffer_rsrc((void*)x, 0, IMG * ROWB, 0x00020000);
    c.ro = __builtin_amdgcn_make_buffer_rsrc(out, 0, IMG * ROWB, 0x00020000);

    {
        const unsigned slot = wv * 55 + lane;
        unsigned spx, sc;
        asm("v_mul_u32_u24 %0, 0x13b2, %2\n\tv_lshrrev_b32 %0, 16, %0\n\tv_mad_i32_i24 %1, %0, -13, %2"
            : "=&v"(spx), "=&v"(sc) : "v"(slot));
        const bool keep = sc < 12 && (unsigned)(x0 - 1) + spx < IMG;
        const unsigned xoff = (unsigned)((flip ? y0 + TH : y0 - 1) * ROWB + (x0 - 1) * 192) + ((slot - spx) << 4);
        c.voff = keep ? xoff : OOB;
    }
    c.xmask = wv < 11 ? (1ull << 55) - 1 : (1ull << 45) - 1;
    c.m0x = ldsb + wv * 880;
    asm volatile("s_barrier" ::: "memory");
    asm volatile("s_mov_b32 m0, %1\n\ts_mov_b64 exec, %3\n\t"
                 "buffer_load_dwordx4 %0, %2, 0 offen lds\n\t"
                 "s_add_u32 m0, m0, %5\n\tv_add_u32 %0, %4, %0\n\t"
                 "buffer_load_dwordx4 %0, %2, 0 offen lds\n\t"
                 "s_add_u32 m0, m0, %5\n\tv_add_u32 %0, %4, %0\n\t"
                 "buffer_load_dwordx4 %0, %2, 0 offen lds\n\t"
                 "s_add_u32 m0, m0, %5\n\tv_add_u32 %0, %4, %0\n\t"
                 "buffer_load_dwordx4 %0, %2, 0 offen lds\n\t"
                 "v_add_u32 %0, %4, %0\n\t"
                 "s_mov_b64 exec, -1"
                 : "+v"(c.voff) : "s"(c.m0x), "s"(c.rx), "s"(c.xmask), "s"(c.ystep), "i"(LROW) : "memory", "scc");

    int fl = flip, wg = wv & 3;
    asm volatile("" : "+v"(lane), "+s"(fl), "+s"(wg));
    const int q = lane >> 4, pp = lane & 3;
    const int wdy = fl ? -13824 : 13824;
    int aL[3][2], aH[2][2], zA[7];
    {
        const unsigned q8 = (lane >> 1) & 24;
        const int wl = (WOFF + wg * 1152 + (fl ? 2 * 13824 : 0)) + ((lane >> 2) & 3) * 48;
        const int t0 = ((0x180d0200u >> q8) & 0xff) * 384, t1 = ((0x190e0c01u >> q8) & 0xff) * 384;
        const int up = pp >> 1;
        const int lo = wl + pp * 8;
        const int hi = wl + 32 + (pp & 1) * 8 - up * wdy;
        const int t2 = 2 * 4608 + 2 * 384 + q * wdy;
        aL[0][0] = lo + t0; aL[0][1] = lo + t1;
        aL[1][0] = aL[0][0] + wdy; aL[1][1] = aL[0][1] + wdy;
        aL[2][0] = aL[1][0] + wdy; aL[2][1] = aL[1][1] + wdy;
        const int h0 = hi + t0, h1 = hi + t1;
        aH[0][0] = h0 + wdy; aH[0][1] = h1 + wdy;
        aH[1][0] = aH[0][0] + wdy; aH[1][1] = aH[0][1] + wdy;
        zA[0] = q < 3 ? lo + t2 : ZOFF;
        zA[1] = q > 0 ? lo + t2 - wdy : ZOFF;
        zA[2] = up ? ZOFF : h0;
        zA[3] = up ? ZOFF : h1;
        zA[4] = up ? aH[1][0] + wdy : ZOFF;
        zA[5] = up ? aH[1][1] + wdy : ZOFF;
        zA[6] = (unsigned)(q - up) < 3 ? hi + t2 : ZOFF;
    }
    asm volatile("" : "+v"(zA[0]), "+v"(zA[1]), "+v"(zA[2]), "+v"(zA[3]), "+v"(zA[4]), "+v"(zA[5]), "+v"(zA[6]));
    asm volatile("" : "+v"(aL[0][0]), "+v"(aL[0][1]), "+v"(aL[1][0]), "+v"(aL[1][1]), "+v"(aL[2][0]), "+v"(aL[2][1]),
                      "+v"(aH[0][0]), "+v"(aH[0][1]), "+v"(aH[1][0]), "+v"(aH[1][1]));

    asm volatile("s_waitcnt vmcnt(4)\n\ts_barrier" ::: "memory");
#pragma unroll
    for (int f = 0; f < 2; ++f) {
        c.AL[0][f] = trfrag(smem + aL[0][f]);
        c.AH[0][f] = trfrag(smem + zA[2 + f]);
        c.AL[1][f] = trfrag(smem + aL[1][f]);
        c.AH[1][f] = trfrag(smem + aH[0][f]);
    }
#pragma unroll
    for (int f = 0; f < 2; ++f) {
        c.AL[2][f] = trfrag(smem + aL[2][f]);
        c.AH[2][f] = trfrag(smem + aH[1][f]);
        c.AH[3][f] = trfrag(smem + zA[4 + f]);
    }
    c.AL2[0] = trfrag(smem + zA[0]);
    c.AL2[1] = trfrag(smem + zA[1]);
    c.AH2 = trfrag(smem + zA[6]);
    asm volatile("" : "+v"(lane) :: "memory");
    load_row(c, 4);
    load_row(c, 5);

    {
        const int g = wv & 3, ct = wv >> 2;
        const int n = lane & 15, qb = lane >> 4;
        const unsigned q8 = (lane >> 1) & 24;
        const int px = 2 * (n & 3) + 8 * (n >> 3) + (((n >> 2) ^ (n >> 3)) & 1);
        const int pbase = ((ct * 16 + px) * 13 + 3 * g) * 16;
        c.pF0 = smem + pbase + ((0x1a0e0200u >> q8) & 0xff) * 16;
        c.pF1 = smem + pbase + ((0x1b0f0d01u >> q8) & 0xff) * 16;
        c.pF2 = smem + pbase + 28 * 16 + qb * LROW;
        c.swL = smem + pbase + 8 * qb;
        c.swH = smem + pbase + 32 + 8 * (qb & 1) + (qb >> 1) * LROW;
        const int rj = lane < 48 ? lane : 47;
        const int rpx = (rj * 43) >> 9;
        c.srd = smem + ((wv * 4 + rpx) * 13 + (rj - rpx * 12)) * 16;
        c.ovoff = lane < 48 ? (unsigned)((x0 + wv * 4) * 192 + lane * 16) : OOB;
        c.osoff = (flip ? y0 + TH - 1 : y0) * ROWB;
    }

    asm volatile("s_waitcnt vmcnt(2)\n\ts_barrier" ::: "memory");
    load_row(c, 6);
    c.Ra[0] = lds_frag(c.pF0); c.Rb[0] = lds_frag(c.pF1);
    c.Ra[1] = lds_frag(c.pF0 + LROW); c.Rb[1] = lds_frag(c.pF1 + LROW);
    load_row(c, 7);

    pair_step<0>(c); pair_step<1>(c); pair_step<2>(c); pair_step<3>(c); pair_step<4>(c); pair_step<5>(c);

    static_assert(st_done(NP - 2) >= 2, "slots 0 and 1 must be drained");
    stage(c.swL, c.pL0);
    stage(c.swL + LROW, c.pL1);
    stage(c.swH, c.pH);
    asm volatile("s_waitcnt lgkmcnt(0)\n\ts_barrier" ::: "memory");
    constexpr int S0 = st_done(NP - 1), NS = TH - S0;
    u32x4 v[NS];
#pragma unroll
    for (int k = 0; k < NS; ++k) v[k] = lds_u32x4(c.srd + (S0 + k >= TH - 2 ? S0 + k - (TH - 2) : S0 + k) * LROW);
#pragma unroll
    for (int k = 0; k < NS; ++k) store_row(c, v[k]);
}

extern "C" void kernel_launch(void* const* d_in, const int* in_sizes, int n_in,
                              void* d_out, int out_size, void* d_ws, size_t ws_size,
                              hipStream_t stream) {
    gconv_kernel<<<256, 768, 0, stream>>>(d_in[0], d_in[1], d_out);
}
```

```cpp
#include <hip/hip_runtime.h>
#include <hip/hip_bf16.h>

typedef __attribute__((ext_vector_type(8))) __bf16 bf16x8;
typedef __attribute__((ext_vector_type(4))) __bf16 bf16x4;
typedef __attribute__((ext_vector_type(2))) __bf16 bf16x2;
typedef __attribute__((ext_vector_type(4))) short s16x4;
typedef __attribute__((ext_vector_type(4))) float f32x4;
typedef __attribute__((ext_vector_type(2))) float f32x2;
typedef __attribute__((ext_vector_type(4))) unsigned u32x4;
typedef __attribute__((ext_vector_type(2))) unsigned u32x2;
typedef __attribute__((address_space(3))) char lds_char;

constexpr int IMG = 384;
constexpr int ROWB = IMG * 192;
constexpr int TW = 48, TH = 12;
constexpr int NROW = TH + 2;
constexpr int NP = TH / 2;
constexpr int LROW = 650 * 16;
constexpr int WBYTES = 9 * 4 * 24 * 24 * 2;
constexpr int WOFF = 10 * LROW;
constexpr int ZOFF = WOFF + WBYTES;
constexpr int LDS_BYTES = WOFF + 48 * 1024;
constexpr unsigned OOB = 0x80000000u;
constexpr int ST_NT = 2;

constexpr int ST[NP] = {0, 0, 0, 4, 2, 2};
constexpr int st_done(int p) { int d = 0; for (int s = 0; s <= p; ++s) d += ST[s]; return d; }
constexpr int st_first(int p) { return p == 0 ? 0 : st_done(p - 1); }
constexpr int mid_row(int p) { return p < 3 ? 8 + 2 * p : -1; }
constexpr int end_row(int p) { return p < 3 ? 9 + 2 * p : -1; }
constexpr int PAUSE_A[NP] = {3, 3, 1, 1, 1, 1};
constexpr int PAUSE_B[NP] = {3, 3, 0, 0, 0, 0};
constexpr int younger(int p) {
    const int need = 2 * p + 5;
    int cnt = 0;
    for (int r = need + 1; r < 8; ++r) ++cnt;
    bool seen = need < 8;
    for (int s = 0; s <= p; ++s) {
        if (mid_row(s) >= 0) { if (seen) ++cnt; if (mid_row(s) == need) seen = true; }
        if (seen) cnt += ST[s];
        if (end_row(s) >= 0) { if (seen) ++cnt; if (end_row(s) == need) seen = true; }
    }
    return cnt;
}

__device__ __forceinline__ void dma16(unsigned m0v, unsigned voff, __amdgpu_buffer_rsrc_t rs, unsigned long long mask) {
    asm volatile("s_mov_b32 m0, %0\n\ts_mov_b64 exec, %3\n\tbuffer_load_dwordx4 %1, %2, 0 offen lds\n\ts_mov_b64 exec, -1"
                 :: "s"(m0v), "v"(voff), "s"(rs), "s"(mask) : "memory");
}
__device__ __forceinline__ bf16x8 lds_frag(const lds_char* p) {
    return *(const __attribute__((address_space(3))) bf16x8*)p;
}
__device__ __forceinline__ u32x4 lds_u32x4(const lds_char* p) {
    return *(const __attribute__((address_space(3))) u32x4*)p;
}
__device__ __forceinline__ bf16x4 trread(const lds_char* p) {
    return __builtin_bit_cast(bf16x4, __builtin_amdgcn_ds_read_tr16_b64_v4i16((__attribute__((address_space(3))) s16x4*)p));
}
__device__ __forceinline__ bf16x8 trfrag(const lds_char* p) {
    return __builtin_shufflevector(trread(p), trread(p + 192), 0, 1, 2, 3, 4, 5, 6, 7);
}
__device__ __forceinline__ f32x4 mma(bf16x8 a, bf16x8 b, f32x4 c) {
    return __builtin_amdgcn_mfma_f32_16x16x32_bf16(a, b, c, 0, 0, 0);
}
__device__ __forceinline__ unsigned pk(float a, float b) {
    f32x2 v = {a, b};
    return __builtin_bit_cast(unsigned, __builtin_convertvector(v, bf16x2));
}
__device__ __forceinline__ void stage(lds_char* p, f32x4 a) {
    *(__attribute__((address_space(3))) u32x2*)p = u32x2{pk(a[0], a[1]), pk(a[2], a[3])};
}

struct Ctx {
    bf16x8 AL[3][2], AL2[2];
    bf16x8 AH[4][2], AH2;
    bf16x8 Ra[4], Rb[4];
    f32x4 pL0, pL1, pH;
    const lds_char *pF0, *pF1, *pF2, *srd;
    lds_char *swL, *swH;
    unsigned voff, ovoff, m0x;
    int osoff, ystep;
    unsigned long long xmask;
    __amdgpu_buffer_rsrc_t rx, ro;
};

__device__ __forceinline__ void load_row(Ctx& c, int r) { dma16(c.m0x + r * LROW, c.voff, c.rx, c.xmask); c.voff += c.ystep; }
__device__ __forceinline__ void store_row(Ctx& c, u32x4 v) {
    __builtin_amdgcn_raw_buffer_store_b128(v, c.ro, c.ovoff, c.osoff, ST_NT);
    c.osoff += c.ystep;
}

template <int P> __device__ __forceinline__ void pair_step(Ctx& c) {
    constexpr int O = 2 * P;
    constexpr int ra = O % 4, rb = (O + 1) % 4, rc = (O + 2) % 4, rd = (O + 3) % 4;
    c.Ra[rc] = lds_frag(c.pF0 + (O + 2) * LROW);
    c.Rb[rc] = lds_frag(c.pF1 + (O + 2) * LROW);
    c.Ra[rd] = lds_frag(c.pF0 + (O + 3) * LROW);
    c.Rb[rd] = lds_frag(c.pF1 + (O + 3) * LROW);
    const bf16x8 F2 = lds_frag(c.pF2 + O * LROW);
    if constexpr (P >= 1) {
        stage(c.swL + (O - 2) * LROW, c.pL0);
        stage(c.swL + (O - 1) * LROW, c.pL1);
        stage(c.swH + (O - 2) * LROW, c.pH);
    }
    constexpr int NS = ST[P], S0 = st_first(P);
    static_assert(S0 + NS <= (P >= 1 ? 2 * P - 2 : 0), "stored rows must have been staged before the last barrier");
    u32x4 ov[NS > 0 ? NS : 1];
#pragma unroll
    for (int k = 0; k < NS; ++k) ov[k] = lds_u32x4(c.srd + (S0 + k) * LROW);

    if constexpr (PAUSE_A[P] > 0) __builtin_amdgcn_s_sleep(PAUSE_A[P]);
    f32x4 l0 = {0, 0, 0, 0}, l1 = {0, 0, 0, 0}, h = {0, 0, 0, 0};
    l0 = mma(c.AL[0][0], c.Ra[ra], l0); h = mma(c.AH[0][0], c.Ra[ra], h);
    l1 = mma(c.AL[0][0], c.Ra[rb], l1);
    l0 = mma(c.AL[0][1], c.Rb[ra], l0); h = mma(c.AH[0][1], c.Rb[ra], h);
    l1 = mma(c.AL[0][1], c.Rb[rb], l1);
    l0 = mma(c.AL[1][0], c.Ra[rb], l0); h = mma(c.AH[1][0], c.Ra[rb], h);
    l0 = mma(c.AL[1][1], c.Rb[rb], l0); h = mma(c.AH[1][1], c.Rb[rb], h);
    if constexpr (mid_row(P) >= 0 || NS > 0) {
        asm volatile("" : "+v"(l0), "+v"(l1), "+v"(h));
        if constexpr (mid_row(P) >= 0) load_row(c, mid_row(P));
        if constexpr (NS > 0) {
#pragma unroll
            for (int k = 0; k < NS; ++k) store_row(c, ov[k]);
            asm volatile("" ::: "memory");
        }
    }
    if constexpr (PAUSE_B[P] > 0) { asm volatile("" : "+v"(l0), "+v"(l1), "+v"(h)); __builtin_amdgcn_s_sleep(PAUSE_B[P]); }
    l1 = mma(c.AL[1][0], c.Ra[rc], l1); l0 = mma(c.AL[2][0], c.Ra[rc], l0); h = mma(c.AH[2][0], c.Ra[rc], h);
    l1 = mma(c.AL[1][1], c.Rb[rc], l1); l0 = mma(c.AL[2][1], c.Rb[rc], l0); h = mma(c.AH[2][1], c.Rb[rc], h);
    l1 = mma(c.AL[2][0], c.Ra[rd], l1); h = mma(c.AH[3][0], c.Ra[rd], h);
    l1 = mma(c.AL[2][1], c.Rb[rd], l1); h = mma(c.AH[3][1], c.Rb[rd], h);
    l0 = mma(c.AL2[0], F2, l0); l1 = mma(c.AL2[1], F2, l1); h = mma(c.AH2, F2, h);
    asm volatile("" : "+v"(l0), "+v"(l1), "+v"(h));
    c.pL0 = l0; c.pL1 = l1; c.pH = h;
    if constexpr (end_row(P) >= 0) load_row(c, end_row(P));
    if constexpr (P + 1 < NP) {
        static_assert(younger(P) < 64, "vmcnt range");
        asm volatile("s_waitcnt vmcnt(%0) lgkmcnt(0)\n\ts_barrier" :: "i"(younger(P)) : "memory");
        __builtin_amdgcn_sched_barrier(0);
    }
}

__global__ __launch_bounds__(768) void gconv_kernel(const void* __restrict__ x, const void* __restrict__ w,
                                                    void* __restrict__ out) {
    __shared__ __attribute__((aligned(16))) char smem_[LDS_BYTES];
    lds_char* smem = (lds_char*)smem_;
    const unsigned ldsb = (unsigned)(size_t)smem;
    Ctx c;

    const int tid = threadIdx.x;
    int lane = tid & 63;
    const int wv = __builtin_amdgcn_readfirstlane(tid >> 6);

    {
        unsigned wm0 = ldsb + WOFF + wv * 4096;
        unsigned wvo = (unsigned)(wv * 4096 + lane * 16);
        asm volatile("" : "+s"(wm0), "+v"(wvo));
        const __amdgpu_buffer_rsrc_t rw = __builtin_amdgcn_make_buffer_rsrc((void*)w, 0, WBYTES, 0x00020000);
        asm volatile("s_mov_b32 m0, %0\n\ts_nop 0\n\t"
                     "buffer_load_dwordx4 %1, %2, 0 offen lds\n\t"
                     "buffer_load_dwordx4 %1, %2, 0 offen offset:1024 lds\n\t"
                     "buffer_load_dwordx4 %1, %2, 0 offen offset:2048 lds\n\t"
                     "buffer_load_dwordx4 %1, %2, 0 offen offset:3072 lds"
                     :: "s"(wm0), "v"(wvo), "s"(rw) : "memory");
    }
    int bid = blockIdx.x;
    asm volatile("" : "+s"(bid));

    const int trow = (bid & 7) * 4 + (bid >> 6);
    const int x0 = ((bid >> 3) & 7) * TW, y0 = trow * TH;
    const bool flip = !(trow & 1);
    c.ystep = flip ? -ROWB : ROWB;
    c.rx = __builtin_amdgcn_make_buffer_rsrc((void*)x, 0, IMG * ROWB, 0x00020000);
    c.ro = __builtin_amdgcn_make_buffer_rsrc(out, 0, IMG * ROWB, 0x00020000);

    {
        const unsigned slot = wv * 55 + lane;
        unsigned spx, sc;
        asm("v_mul_u32_u24 %0, 0x13b2, %2\n\tv_lshrrev_b32 %0, 16, %0\n\tv_mad_i32_i24 %1, %0, -13, %2"
            : "=&v"(spx), "=&v"(sc) : "v"(slot));
        const bool keep = sc < 12 && (unsigned)(x0 - 1) + spx < IMG;
        const unsigned xoff = (unsigned)((flip ? y0 + TH : y0 - 1) * ROWB + (x0 - 1) * 192) + ((slot - spx) << 4);
        c.voff = keep ? xoff : OOB;
    }
    c.xmask = wv < 11 ? (1ull << 55) - 1 : (1ull << 45) - 1;
    c.m0x = ldsb + wv * 880;
    asm volatile("s_barrier" ::: "memory");
    asm volatile("s_mov_b32 m0, %1\n\ts_mov_b64 exec, %3\n\t"
                 "buffer_load_dwordx4 %0, %2, 0 offen lds\n\t"
                 "s_add_u32 m0, m0, %5\n\tv_add_u32 %0, %4, %0\n\t"
                 "buffer_load_dwordx4 %0, %2, 0 offen lds\n\t"
                 "s_add_u32 m0, m0, %5\n\tv_add_u32 %0, %4, %0\n\t"
                 "buffer_load_dwordx4 %0, %2, 0 offen lds\n\t"
                 "s_add_u32 m0, m0, %5\n\tv_add_u32 %0, %4, %0\n\t"
                 "buffer_load_dwordx4 %0, %2, 0 offen lds\n\t"
                 "v_add_u32 %0, %4, %0\n\t"
                 "s_mov_b64 exec, -1"
                 : "+v"(c.voff) : "s"(c.m0x), "s"(c.rx), "s"(c.xmask), "s"(c.ystep), "i"(LROW) : "memory", "scc");

    int fl = flip, wg = wv & 3;
    asm volatile("" : "+v"(lane), "+s"(fl), "+s"(wg));
    const int q = lane >> 4, pp = lane & 3;
    const int wdy = fl ? -13824 : 13824;
    int aL[3][2], aH[2][2], zA[7];
    {
        const unsigned q8 = (lane >> 1) & 24;
        const int wl = (WOFF + wg * 1152 + (fl ? 2 * 13824 : 0)) + ((lane >> 2) & 3) * 48;
        const int t0 = ((0x180d0200u >> q8) & 0xff) * 384, t1 = ((0x190e0c01u >> q8) & 0xff) * 384;
        const int up = pp >> 1;
        const int lo = wl + pp * 8;
        const int hi = wl + 32 + (pp & 1) * 8 - up * wdy;
        const int t2 = 2 * 4608 + 2 * 384 + q * wdy;
        aL[0][0] = lo + t0; aL[0][1] = lo + t1;
        aL[1][0] = aL[0][0] + wdy; aL[1][1] = aL[0][1] + wdy;
        aL[2][0] = aL[1][0] + wdy; aL[2][1] = aL[1][1] + wdy;
        const int h0 = hi + t0, h1 = hi + t1;
        aH[0][0] = h0 + wdy; aH[0][1] = h1 + wdy;
        aH[1][0] = aH[0][0] + wdy; aH[1][1] = aH[0][1] + wdy;
        zA[0] = q < 3 ? lo + t2 : ZOFF;
        zA[1] = q > 0 ? lo + t2 - wdy : ZOFF;
        zA[2] = up ? ZOFF : h0;
        zA[3] = up ? ZOFF : h1;
        zA[4] = up ? aH[1][0] + wdy : ZOFF;
        zA[5] = up ? aH[1][1] + wdy : ZOFF;
        zA[6] = (unsigned)(q - up) < 3 ? hi + t2 : ZOFF;
    }
    asm volatile("" : "+v"(zA[0]), "+v"(zA[1]), "+v"(zA[2]), "+v"(zA[3]), "+v"(zA[4]), "+v"(zA[5]), "+v"(zA[6]));
    asm volatile("" : "+v"(aL[0][0]), "+v"(aL[0][1]), "+v"(aL[1][0]), "+v"(aL[1][1]), "+v"(aL[2][0]), "+v"(aL[2][1]),
                      "+v"(aH[0][0]), "+v"(aH[0][1]), "+v"(aH[1][0]), "+v"(aH[1][1]));

    asm volatile("s_waitcnt vmcnt(4)\n\ts_barrier" ::: "memory");
#pragma unroll
    for (int f = 0; f < 2; ++f) {
        c.AL[0][f] = trfrag(smem + aL[0][f]);
        c.AH[0][f] = trfrag(smem + zA[2 + f]);
        c.AL[1][f] = trfrag(smem + aL[1][f]);
        c.AH[1][f] = trfrag(smem + aH[0][f]);
    }
    asm volatile("" ::: "memory");
    load_row(c, 4);
#pragma unroll
    for (int f = 0; f < 2; ++f) {
        c.AL[2][f] = trfrag(smem + aL[2][f]);
        c.AH[2][f] = trfrag(smem + aH[1][f]);
        c.AH[3][f] = trfrag(smem + zA[4 + f]);
    }
    c.AL2[0] = trfrag(smem + zA[0]);
    c.AL2[1] = trfrag(smem + zA[1]);
    c.AH2 = trfrag(smem + zA[6]);
    asm volatile("" : "+v"(lane) :: "memory");
    load_row(c, 5);
    load_row(c, 6);

    {
        const int g = wv & 3, ct = wv >> 2;
        const int n = lane & 15, qb = lane >> 4;
        const unsigned q8 = (lane >> 1) & 24;
        const int px = 2 * (n & 3) + 8 * (n >> 3) + (((n >> 2) ^ (n >> 3)) & 1);
        const int pbase = ((ct * 16 + px) * 13 + 3 * g) * 16;
        c.pF0 = smem + pbase + ((0x1a0e0200u >> q8) & 0xff) * 16;
        c.pF1 = smem + pbase + ((0x1b0f0d01u >> q8) & 0xff) * 16;
        c.pF2 = smem + pbase + 28 * 16 + qb * LROW;
        c.swL = smem + pbase + 8 * qb;
        c.swH = smem + pbase + 32 + 8 * (qb & 1) + (qb >> 1) * LROW;
        const int rj = lane < 48 ? lane : 47;
        const int rpx = (rj * 43) >> 9;
        c.srd = smem + ((wv * 4 + rpx) * 13 + (rj - rpx * 12)) * 16;
        c.ovoff = lane < 48 ? (unsigned)((x0 + wv * 4) * 192 + lane * 16) : OOB;
        c.osoff = (flip ? y0 + TH - 1 : y0) * ROWB;
    }

    asm volatile("s_waitcnt vmcnt(3)\n\ts_barrier" ::: "memory");
    c.Ra[0] = lds_frag(c.pF0); c.Rb[0] = lds_frag(c.pF1);
    c.Ra[1] = lds_frag(c.pF0 + LROW); c.Rb[1] = lds_frag(c.pF1 + LROW);
    load_row(c, 7);

    pair_step<0>(c); pair_step<1>(c); pair_step<2>(c); pair_step<3>(c); pair_step<4>(c); pair_step<5>(c);

    static_assert(st_done(NP - 2) >= 2, "slots 0 and 1 must be drained");
    stage(c.swL, c.pL0);
    stage(c.swL + LROW, c.pL1);
    stage(c.swH, c.pH);
    asm volatile("s_waitcnt lgkmcnt(0)\n\ts_barrier" ::: "memory");
    constexpr int S0 = st_done(NP - 1), NS = TH - S0;
    u32x4 v[NS];
#pragma unroll
    for (int k = 0; k < NS; ++k) v[k] = lds_u32x4(c.srd + (S0 + k >= TH - 2 ? S0 + k - (TH - 2) : S0 + k) * LROW);
#pragma unroll
    for (int k = 0; k < NS; ++k) store_row(c, v[k]);
}

extern "C" void kernel_launch(void* const* d_in, const int* in_sizes, int n_in,
                              void* d_out, int out_size, void* d_ws, size_t ws_size,
                              hipStream_t stream) {
    gconv_kernel<<<256, 768, 0, stream>>>(d_in[0], d_in[1], d_out);
}
```
